```python
import torch

def _softmax(x, axis=-1):
    return torch.softmax(x, dim=axis)
def _gelu(x):
    return torch.nn.functional.gelu(x, approximate="tanh")
def _silu(x):
    return torch.nn.functional.silu(x)
def _sigmoid(x):
    return torch.sigmoid(x)
def _layernorm(x, axis=-1, eps=1e-5):
    m = x.mean(dim=axis, keepdim=True)
    v = x.var(dim=axis, keepdim=True, unbiased=False)
    return (x - m) / torch.sqrt(v + eps)
def _scatter_add(tgt, idx, src, axis=0):
    return tgt.clone().index_add_(axis, idx, src)
def _segment_sum(src, seg, n):
    out = torch.zeros((n,) + src.shape[1:], dtype=src.dtype)
    return out.index_add_(0, seg, src)
def _pad(x, pw):
    flat = tuple(v for pair in reversed(pw) for v in pair)
    return torch.nn.functional.pad(x, flat)

kernel_name = 'parallel_sum__sliding_block_cached__dense_activation_mish__decode'

def setup_inputs():

    _w = (lambda a: (lambda t: t.to(torch.bfloat16) if t.is_floating_point() else t)(torch.as_tensor(a)))
    def _A_setup():
        B, Sq, D = 4, 1, 448
        Hq, Hkv, Sk, Dh = 16, 8, 4096, 256
        assert Hq % Hkv == 0, (Hq, Hkv)
        return {
            "x": _w((lambda *s: torch.randn(*s, dtype=torch.bfloat16) * 0.05)(B, Sq, D)),
            "k_cache": _w((lambda *s: torch.randn(*s, dtype=torch.bfloat16) * 1.6)(B, Hkv, Sk, Dh)),
            "v_cache": _w((lambda *s: torch.randn(*s, dtype=torch.bfloat16) * 0.4)(B, Hkv, Sk, Dh)),
            "wq": _w((lambda *s: torch.randn(*s, dtype=torch.bfloat16) * 1.6)(D, Hq * Dh) * (D ** -0.5)),
            "wo": _w((lambda *s: torch.randn(*s, dtype=torch.bfloat16) * 0.4)(Hq * Dh, D) * ((Hq * Dh) ** -0.5)),
        }
    _A_in = _A_setup()

    def _B_setup():
        B, S, D, F = 4, 1, 448, 1792
        return {
            "x": _w((lambda *s: torch.randn(*s, dtype=torch.bfloat16) * 0.05)(B, S, D)),
            "w1": _w((lambda *s: torch.randn(*s, dtype=torch.bfloat16) * 0.8)(D, F) * (D ** -0.5)),
            "b1": _w((lambda *s: torch.randn(*s, dtype=torch.bfloat16) * 0.2)(F)),
            "w2": _w((lambda *s: torch.randn(*s, dtype=torch.bfloat16) * 0.1)(F, D) * (F ** -0.5)),
        }
    _B_in = _B_setup()

    return {
        "A_x": _A_in["x"],
        "A_k_cache": _A_in["k_cache"],
        "A_v_cache": _A_in["v_cache"],
        "A_wq": _A_in["wq"],
        "A_wo": _A_in["wo"],
        "B_x": _B_in["x"],
        "B_w1": _B_in["w1"],
        "B_b1": _B_in["b1"],
        "B_w2": _B_in["w2"],
    }

def reference(A_x, A_k_cache, A_v_cache, A_wq, A_wo, B_x, B_w1, B_b1, B_w2):
    def _A_ref(x, k_cache, v_cache, wq, wo):
        B, Sq, D = x.shape
        Hq, Dh = 16, 256
        Hkv, Sk = k_cache.shape[1], k_cache.shape[2]
        W = min(4096, max(256, Sk // 4))
        rep = Hq // Hkv
        q = (lambda x, *d: x.permute(*d))((x @ wq).reshape(B, Sq, Hq, Dh), 0, 2, 1, 3)
        k = (lambda x, n, axis: torch.repeat_interleave(x, n, dim=axis))(k_cache[:, :, Sk - W:, :], rep, axis=1)
        v = (lambda x, n, axis: torch.repeat_interleave(x, n, dim=axis))(v_cache[:, :, Sk - W:, :], rep, axis=1)
        scores = torch.einsum("bhqd,bhkd->bhqk", q, k) * (Dh ** -0.5)
        p = _softmax(scores, axis=-1)
        o = torch.einsum("bhqk,bhkd->bhqd", p, v)
        return (lambda x, *d: x.permute(*d))(o, 0, 2, 1, 3).reshape(B, Sq, Hq * Dh) @ wo

    def _B_ref(x, w1, b1, w2):
        h = torch.einsum("bsd,df->bsf", x, w1) + b1
        sp = torch.log1p(torch.exp(-torch.abs(h))) + torch.where(h > 0, h, torch.zeros_like(h))
        act = h * torch.tanh(sp)
        return torch.einsum("bsf,fd->bsd", act, w2)

    x = A_x
    xn = _layernorm(x, axis=-1)
    return x + _A_ref(xn, A_k_cache, A_v_cache, A_wq, A_wo) + _B_ref(xn, B_w1, B_b1, B_w2)
```

```cpp
#include <hip/hip_runtime.h>
#include <stdint.h>

typedef unsigned short u16;
typedef unsigned int u32;
typedef unsigned long long u64;
typedef u32 v4u __attribute__((ext_vector_type(4)));
typedef u32 v2u __attribute__((ext_vector_type(2)));
typedef float f32x4 __attribute__((ext_vector_type(4)));
typedef float f32x2 __attribute__((ext_vector_type(2)));
typedef short v4s __attribute__((ext_vector_type(4)));
typedef __bf16 v8bf __attribute__((ext_vector_type(8)));
typedef __bf16 v2bf __attribute__((ext_vector_type(2)));
#define LAS __attribute__((address_space(3)))
#define GAS __attribute__((address_space(1)))

constexpr int DM = 448, NQ = 4096, SK = 4096, DH = 256, WIN = 1024, FF = 1792;
constexpr u32 WS_STRIDE = 192u << 10;
constexpr u32 WS_EPOCH = 0, WS_R0 = 65536, WR_ST = 0, WR_QP = 4096, WR_HP = WR_QP + 32768, WR_OP = WR_HP + 28672, WR_P3 = WR_OP + 65536, WR_END = WR_P3 + 57344;
constexpr u32 WS_TOTAL = WS_R0 + 7 * WS_STRIDE + WR_END;
#define WREG(i) ((int)(WS_R0 + (u32)(i) * WS_STRIDE))
constexpr u32 L_FLAG = 0, L_H = 240  , L_XN = 256, L_Q = 3840, L_P = 7936, L_MX = 8448, L_LS = 8480, L_ACT = 8512, L_OF = 8896,
              L_WQ = 12992, L_W1 = 27328, L_WO = 34816, L_END = 34816 + 24576, L_ALLOC = 86016;
enum { F_CLR = 0, F_XN = 1, F_KISS = 2, F_Q = 3, F_MAX = 4, F_P = 5, F_VISS = 6, F_W = 7, F_ACT = 8, F_OPUB = 10, F_S1 = 12 };
constexpr int AUX_SC1 = 16;
constexpr u32 GSPIN = 1u << 13, LSPIN = 1u << 20;
constexpr int WINK = 6, WINV = 6;
constexpr int KEARLY = 4;
constexpr int PDEL3 = 24;

__device__ __forceinline__ float asf(u32 v) { return __builtin_bit_cast(float, v); }
__device__ __forceinline__ u32 asu(float v) { return __builtin_bit_cast(u32, v); }
__device__ __forceinline__ float bf2f_lo(u32 v) { return __builtin_bit_cast(float, v << 16); }
__device__ __forceinline__ float bf2f_hi(u32 v) { return __builtin_bit_cast(float, v & 0xFFFF0000u); }
__device__ __forceinline__ u32 pk_bf16(float lo, float hi) { f32x2 t = {lo, hi}; return __builtin_bit_cast(u32, __builtin_convertvector(t, v2bf)); }
__device__ __forceinline__ f32x4 mfma(v4u a, v4u b, f32x4 c) {
  return __builtin_amdgcn_mfma_f32_16x16x32_bf16(__builtin_bit_cast(v8bf, a), __builtin_bit_cast(v8bf, b), c, 0, 0, 0);
}
template <int CTRL> __device__ __forceinline__ float dpp(float x) {
  return __builtin_bit_cast(float, __builtin_amdgcn_mov_dpp(__builtin_bit_cast(int, x), CTRL, 0xf, 0xf, true));
}
constexpr int XOR1 = 0xB1, XOR2 = 0x4E, ROR4 = 0x124, ROR8 = 0x128;
__device__ __forceinline__ float xrow_sum(float x) {
  auto s = __builtin_amdgcn_permlane16_swap(__builtin_bit_cast(u32, x), __builtin_bit_cast(u32, x), false, false);
  x = asf(s[0]) + asf(s[1]);
  auto t = __builtin_amdgcn_permlane32_swap(__builtin_bit_cast(u32, x), __builtin_bit_cast(u32, x), false, false);
  return asf(t[0]) + asf(t[1]);
}
__device__ __forceinline__ float xrow_max(float x) {
  auto s = __builtin_amdgcn_permlane16_swap(__builtin_bit_cast(u32, x), __builtin_bit_cast(u32, x), false, false);
  x = fmaxf(asf(s[0]), asf(s[1]));
  auto t = __builtin_amdgcn_permlane32_swap(__builtin_bit_cast(u32, x), __builtin_bit_cast(u32, x), false, false);
  return fmaxf(asf(t[0]), asf(t[1]));
}
__device__ __forceinline__ float wave_sum(float x) {
  x += dpp<XOR1>(x); x += dpp<XOR2>(x); x += dpp<ROR4>(x); x += dpp<ROR8>(x);
  return xrow_sum(x);
}
__device__ __forceinline__ u32 tagw(float f, u32 tag) { u32 r; asm("v_bfi_b32 %0, %1, %2, %3" : "=v"(r) : "s"(0xFFFu), "v"(tag), "v"(f)); return r; }
__device__ __forceinline__ v4u tag4(f32x4 v, u32 tag) { return v4u{tagw(v[0], tag), tagw(v[1], tag), tagw(v[2], tag), tagw(v[3], tag)}; }
__device__ __forceinline__ u32 or4(v4u v) { return (v.x | v.y | v.z) | v.w; }
#define UNTAG(v, tag) ((v) ^ (tag))
__device__ __forceinline__ v4u tr_frag(LAS unsigned char* p0, LAS unsigned char* p1) {
  const v2u a = __builtin_bit_cast(v2u, __builtin_amdgcn_ds_read_tr16_b64_v4i16((LAS v4s*)p0));
  const v2u b = __builtin_bit_cast(v2u, __builtin_amdgcn_ds_read_tr16_b64_v4i16((LAS v4s*)p1));
  return v4u{a.x, a.y, b.x, b.y};
}
#define LGKM0() asm volatile("s_waitcnt lgkmcnt(0)" ::: "memory")
#define VM0() asm volatile("s_waitcnt vmcnt(0)" ::: "memory")
#define VMW(n) asm volatile("s_waitcnt vmcnt(%0)" ::"n"(n) : "memory")
#define SB() __builtin_amdgcn_sched_barrier(0)
__device__ __forceinline__ void lds_signal(LAS u32* flg, int f, int i, int lane) {
  LGKM0();
  if (lane == 0) *(volatile LAS u32*)(flg + 4 * f + i) = 1u;
}
__device__ __forceinline__ void lds_wait4(LAS u32* flg, int f) {
#pragma unroll 1
  for (u32 it = 0; it < LSPIN; ++it) {
    const v4u v = *(volatile LAS v4u*)(flg + 4 * f);
    if (__builtin_amdgcn_readfirstlane(v.x & v.y & v.z & v.w)) break;
    __builtin_amdgcn_s_sleep(1);
  }
  asm volatile("" ::: "memory");
}
__device__ __forceinline__ void lds_wait4_fast(LAS u32* flg, int f) {
#pragma unroll 1
  for (u32 it = 0; it < LSPIN; ++it) {
    const v4u v = *(volatile LAS v4u*)(flg + 4 * f);
    if (__builtin_amdgcn_readfirstlane(v.x & v.y & v.z & v.w)) break;
  }
  asm volatile("" ::: "memory");
}
__device__ __forceinline__ void lds_waitn(LAS u32* flg, int f, int i) {
#pragma unroll 1
  for (u32 it = 0; it < LSPIN; ++it) {
    const u32 v = *(volatile LAS u32*)(flg + 4 * f + i);
    if (__builtin_amdgcn_readfirstlane(v)) break;
    __builtin_amdgcn_s_sleep(1);
  }
  asm volatile("" ::: "memory");
}
#define MEMFENCE() asm volatile("" ::: "memory")
__device__ __forceinline__ v4u ld_nt(const u16* p) { return __builtin_nontemporal_load((const GAS v4u*)p); }
__device__ __forceinline__ void glds(const u16* src, LAS unsigned char* dst) {
  asm volatile("s_mov_b32 m0, %1\n\ts_nop 0\n\tglobal_load_lds_dwordx4 %0, off nt" ::"v"(src), "s"((u32)(size_t)dst) : "memory");
}

__global__ void __launch_bounds__(576, 3)
fused_decode_kernel(const u16* __restrict__ x, const u16* __restrict__ kc, const u16* __restrict__ vc, const u16* __restrict__ wq,
                    const u16* __restrict__ wo, const u16* __restrict__ w1, const u16* __restrict__ b1, const u16* __restrict__ w2,
                    u16* out, unsigned char* ws) {
  __shared__ __attribute__((aligned(16))) unsigned char smem[L_ALLOC];
  LAS unsigned char* L = (LAS unsigned char*)smem;
  LAS u32* flg = (LAS u32*)(L + L_FLAG);
  const int tid = threadIdx.x, lane = tid & 63, w = __builtin_amdgcn_readfirstlane(tid >> 6);
  const int c = blockIdx.x, s = c & 7, pair = c >> 3, b = pair >> 3, kvh = pair & 7;
  const int g = lane >> 4, r16 = lane & 15;
  const __amdgpu_buffer_rsrc_t rs = __builtin_amdgcn_make_buffer_rsrc((void*)ws, 0, (int)WS_TOTAL, 0x00020000);
  asm volatile("" ::"s"(x), "s"(kc), "s"(vc), "s"(wq), "s"(wo), "s"(w1), "s"(b1), "s"(w2), "s"(out), "s"(ws));
  if (tid < 60) flg[tid] = 0u;

  const int WI = w & 3;
  v4u xr;
  {
    int wa = w, wb_ = w, wc = w;
    asm volatile("" : "+s"(wa));
    if (wa < 4) {
      const u16* xp = x + w * DM + (lane < 56 ? lane : 0) * 8;
      asm volatile("global_load_dwordx4 %0, %1, off" : "=v"(xr) : "v"(xp) : "memory");
    }
    asm volatile("" : "+s"(wb_));
    if (wb_ == 4) {
      const u16* src = wq + (size_t)(112 * b + (lane >> 3)) * NQ + 64 * (8 * kvh + s) + (lane & 7) * 8;
#pragma unroll
      for (int i = 0; i < 14; ++i) glds(src + (size_t)(8 * i) * NQ, L + L_WQ + i * 1024);
    }
    {
      int wd = w; asm volatile("" : "+s"(wd));
      if (wd == 8) {
        u64 pc; u32 len;
        asm volatile("s_getpc_b64 %0\n.Lfdk_pf_here:\n\ts_mov_b32 %1, .Lfdk_code_end-.Lfdk_pf_here" : "=s"(pc), "=s"(len));
        u32 o = ((pair & 7) * 14 + lane) * 128;
        o = o < len ? o : len - 4;
        if (lane < 14) asm volatile("s_mov_b32 m0, %1\n\ts_nop 0\n\tglobal_load_lds_dword %0, off" ::"v"(pc + (u64)(o & ~3u)), "i"(70000) : "memory");
      }
    }
    asm volatile("" : "+s"(wc));
    if (wc == 5 && c < 224) {
      const u16* src = w1 + (size_t)(56 * s + (lane >> 3)) * FF + 64 * pair + (lane & 7) * 8;
#pragma unroll
      for (int i = 0; i < 7; ++i) glds(src + (size_t)(8 * i) * FF, L + L_W1 + i * 1024);
    }
  }
  SB();
  if (w < 4) {
    const u16* kp = kc + ((size_t)pair * SK + (SK - WIN) + s * 128 + 8 * (lane & 7) + 2 * w) * DH + (2 * g + ((lane >> 3) & 1)) * 8;
    v4u K[16];
#define KOFF(i) ((((i) >> 3) + 64 * ((i) & 1)) * DH + (((i) >> 1) & 3) * 64)
#define KDEAL(p) do { _Pragma("unroll") for (int d_ = 0; d_ < 4; ++d_) { const u32 x_ = K[2 * (p)][d_], y_ = K[2 * (p) + 1][d_]; \
      K[2 * (p)][d_] = __builtin_amdgcn_update_dpp(x_, y_, ROR8, 0xf, 0xC, false); K[2 * (p) + 1][d_] = __builtin_amdgcn_update_dpp(y_, x_, ROR8, 0xf, 0x3, false); } } while (0)
    LGKM0();
    __builtin_amdgcn_s_barrier();
    SB();
#pragma unroll
    for (int i = 0; i < KEARLY; ++i) K[i] = ld_nt(kp + KOFF(i));
    SB();
    {
      asm volatile("s_waitcnt vmcnt(%1)" : "+v"(xr) : "n"(KEARLY) : "memory");
      float v[8];
#pragma unroll
      for (int i = 0; i < 4; ++i) { v[2 * i] = bf2f_lo(xr[i]); v[2 * i + 1] = bf2f_hi(xr[i]); }
      float sm = ((v[0] + v[1]) + (v[2] + v[3])) + ((v[4] + v[5]) + (v[6] + v[7]));
      if (lane >= 56) sm = 0.f;
      const float mean = wave_sum(sm) * (1.0f / DM);
      float sq = 0.f;
#pragma unroll
      for (int i = 0; i < 8; ++i) { v[i] -= mean; sq = fmaf(v[i], v[i], sq); }
      if (lane >= 56) sq = 0.f;
      const float rstd = __builtin_amdgcn_rsqf(wave_sum(sq) * (1.0f / DM) + 1e-5f);
      if (lane < 56)
        *(LAS v4u*)(L + L_XN + w * (DM * 2) + lane * 16) = v4u{pk_bf16(v[0] * rstd, v[1] * rstd), pk_bf16(v[2] * rstd, v[3] * rstd),
                                                              pk_bf16(v[4] * rstd, v[5] * rstd), pk_bf16(v[6] * rstd, v[7] * rstd)};
      lds_signal(flg, F_XN, WI, lane);
    }
    SB();
#pragma unroll
    for (int i = KEARLY; i < WINK; ++i) K[i] = ld_nt(kp + KOFF(i));
    SB();
#pragma unroll
    for (int i = WINK; i < 16; ++i) {
      VMW(WINK - 1); K[i] = ld_nt(kp + KOFF(i)); SB();
      if (((i - WINK) & 1) == 1) { KDEAL((i - WINK) >> 1); SB(); }
    }
    const int qoff = WREG(kvh) + (int)WR_QP + ((g * 4 + b) * 128 + 32 * w + r16) * 16;
    v4u q0 = __builtin_amdgcn_raw_buffer_load_b128(rs, qoff, 0, AUX_SC1);
    v4u q1 = __builtin_amdgcn_raw_buffer_load_b128(rs, qoff + 256, 0, AUX_SC1);
    SB();
    lds_signal(flg, F_KISS, WI, lane);
    SB();
#pragma unroll
    for (int p = (16 - WINK) >> 1; p < 8; ++p) KDEAL(p);
    lds_waitn(flg, F_CLR, 0);
    const v4u H = *(LAS v4u*)(L + L_H);
    const u32 tag = H.z;
    {
#pragma unroll 1
      for (u32 it = 0; it < GSPIN; ++it) {
        const bool ok = (q0.x ^ q0.z) == H.x && (q0.y ^ q0.w) == H.y && (q1.x ^ q1.z) == H.x && (q1.y ^ q1.w) == H.y;
        if (__all(ok)) break;
        __builtin_amdgcn_s_sleep(2);
        MEMFENCE();
        q0 = __builtin_amdgcn_raw_buffer_load_b128(rs, qoff, 0, AUX_SC1);
        q1 = __builtin_amdgcn_raw_buffer_load_b128(rs, qoff + 256, 0, AUX_SC1);
      }
      *(LAS v2u*)(L + L_Q + (g * 512 + 128 * w + 4 * r16) * 2) = v2u{q0.x, q0.y};
      *(LAS v2u*)(L + L_Q + (g * 512 + 128 * w + 64 + 4 * r16) * 2) = v2u{q1.x, q1.y};
    }
    lds_signal(flg, F_Q, WI, lane);
    const int kap = 8 * kvh + s, nu = b;
    const u16* lsrc[6];
#pragma unroll
    for (int j = 0; j < 6; ++j) {
      const int i = w + 4 * j, ch = (r16 ^ (2 * w)) & (nu == 3 ? 7 : 15);
      const int r2 = (i - 16) + 8 * g;
      lsrc[j] = j < 4 ? wo + ((64 * kap + i + 16 * g) * DM + 128 * nu + 8 * ch) : w2 + ((28 * kap + (r2 < 28 ? r2 : 27)) * DM + 128 * nu + 8 * ch);
    }
    lds_wait4(flg, F_Q);
    f32x4 sc[2] = {{0.f, 0.f, 0.f, 0.f}, {0.f, 0.f, 0.f, 0.f}};
#pragma unroll
    for (int cs = 0; cs < 4; ++cs)
#pragma unroll
      for (int par = 0; par < 2; ++par) {
        const v4u qf = *(LAS v4u*)(L + L_Q + ((r16 >> 2) * 512 + (lane & 1) * 256 + (8 * cs + 2 * g + par) * 8) * 2);
        sc[0] = mfma(K[2 * cs + par], qf, sc[0]);
        sc[1] = mfma(K[8 + 2 * cs + par], qf, sc[1]);
      }
#pragma unroll
    for (int t = 0; t < 2; ++t)
#pragma unroll
      for (int i = 0; i < 4; ++i) { float v = sc[t][i]; v += dpp<ROR4>(v); v += dpp<ROR8>(v); sc[t][i] = v; }
    float mx = fmaxf(fmaxf(fmaxf(sc[0][0], sc[0][1]), fmaxf(sc[0][2], sc[0][3])), fmaxf(fmaxf(sc[1][0], sc[1][1]), fmaxf(sc[1][2], sc[1][3])));
    mx = xrow_max(mx);
    if (lane < 2) *(LAS float*)(L + L_MX + (w * 2 + lane) * 4) = mx;
    lds_signal(flg, F_MAX, WI, lane);
    lds_wait4(flg, F_MAX);
    {
      LAS float* m4 = (LAS float*)(L + L_MX) + (lane & 1);
      mx = fmaxf(fmaxf(m4[0], m4[2]), fmaxf(m4[4], m4[6]));
    }
    constexpr float C2 = 0.0625f * 1.4426950408889634f;
    const float mneg = -mx * C2;
    float lsum = 0.f;
#pragma unroll
    for (int i = 0; i < 4; ++i) {
      const float p0 = __builtin_amdgcn_exp2f(fmaf(sc[0][i], C2, mneg)), p1 = __builtin_amdgcn_exp2f(fmaf(sc[1][i], C2, mneg));
      lsum += p0 + p1;
      if (r16 < 2) *(LAS u32*)(L + L_P + (r16 * 128 + 8 * (4 * g + i) + 2 * w) * 2) = pk_bf16(p0, p1);
    }
    lsum = xrow_sum(lsum);
    if (lane < 2) *(LAS float*)(L + L_LS + (w * 2 + lane) * 4) = lsum;
    lds_signal(flg, F_P, WI, lane);
    if (w == 0) {
      lds_wait4(flg, F_P);
      if (lane < 2) {
        LAS float* l4 = (LAS float*)(L + L_LS) + lane;
        const float l = (l4[0] + l4[2]) + (l4[4] + l4[6]);
        const u32 m_ = asu(mx * C2), l_ = asu(l);
        __builtin_amdgcn_raw_buffer_store_b128(v4u{m_, l_, m_ ^ H.x, l_ ^ H.y}, rs, WREG(kvh) + (int)WR_ST + ((lane * 4 + b) * 8 + s) * 16, 0, AUX_SC1);
      }
    }
    lds_wait4(flg, F_VISS);
#pragma unroll
    for (int j = 0; j < 6; ++j) glds(lsrc[j], L + L_WO + (w + 4 * j) * 1024);
    const int ooff = WREG(kvh) + (int)WR_OP + ((((s >> 2) * 4 + (s & 3)) * 4 + (r16 >> 2)) * 8 + (r16 & 3) + 4 * (w >> 1)) * 256 + (32 * (w & 1) + 8 * g) * 4;
    int p3off = WREG(kvh) + (int)WR_P3 + ((s * 4 + (r16 >> 2)) * 448 + 128 * nu + 16 * (2 * w + (lane & 1)) + 4 * g) * 4;
    int ofw = (int)L_OF + (w * 64 + lane) * 16;
    asm volatile("" : "+v"(p3off), "+v"(ofw));
    lds_wait4_fast(flg, F_OPUB);
    const int soff = WREG(kvh) + (int)WR_ST + (((s >> 2) * 4 + (r16 >> 2)) * 8 + (r16 & 3)) * 16;
    v4u s0 = __builtin_amdgcn_raw_buffer_load_b128(rs, soff, 0, AUX_SC1);
    v4u s1 = __builtin_amdgcn_raw_buffer_load_b128(rs, soff + 64, 0, AUX_SC1);
    v4u o0 = __builtin_amdgcn_raw_buffer_load_b128(rs, ooff, 0, AUX_SC1);
    v4u o1 = __builtin_amdgcn_raw_buffer_load_b128(rs, ooff + 16, 0, AUX_SC1);
    SB();
    VMW(4);
    lds_signal(flg, F_W, WI, lane);
    lds_wait4(flg, F_W);
    v4u Wf[2][3];
    {
      const int q4 = r16 >> 2, ch0 = 4 * w + ((lane & 3) >> 1);
      const int sl0 = (ch0 ^ (2 * q4)) * 16 + (lane & 1) * 8, sl1 = ((ch0 + 2) ^ (2 * q4)) * 16 + (lane & 1) * 8;
      const u32 ro = (u32)(size_t)L + L_WO + (8 * (g & 1) + q4) * 1024 + (g >> 1) * 256, r2 = (u32)(size_t)L + L_WO + 16384 + q4 * 1024 + g * 256;
      v2u f[12];
      asm volatile("ds_read_b64_tr_b16 %0, %12\n\tds_read_b64_tr_b16 %1, %12 offset:4096\n\tds_read_b64_tr_b16 %2, %12 offset:512\n\tds_read_b64_tr_b16 %3, %12 offset:4608\n\t"
                   "ds_read_b64_tr_b16 %4, %13\n\tds_read_b64_tr_b16 %5, %13 offset:4096\n\t"
                   "ds_read_b64_tr_b16 %6, %14\n\tds_read_b64_tr_b16 %7, %14 offset:4096\n\tds_read_b64_tr_b16 %8, %14 offset:512\n\tds_read_b64_tr_b16 %9, %14 offset:4608\n\t"
                   "ds_read_b64_tr_b16 %10, %15\n\tds_read_b64_tr_b16 %11, %15 offset:4096\n\ts_waitcnt lgkmcnt(0)"
                   : "=&v"(f[0]), "=&v"(f[1]), "=&v"(f[2]), "=&v"(f[3]), "=&v"(f[4]), "=&v"(f[5]), "=&v"(f[6]), "=&v"(f[7]), "=&v"(f[8]), "=&v"(f[9]), "=&v"(f[10]), "=&v"(f[11])
                   : "v"(ro + sl0), "v"(r2 + sl0), "v"(ro + sl1), "v"(r2 + sl1) : "memory");
#pragma unroll
      for (int tt = 0; tt < 2; ++tt)
#pragma unroll
        for (int ks = 0; ks < 3; ++ks) Wf[tt][ks] = v4u{f[6 * tt + 2 * ks].x, f[6 * tt + 2 * ks].y, f[6 * tt + 2 * ks + 1].x, f[6 * tt + 2 * ks + 1].y};
    }
    lds_waitn(flg, F_ACT, 0);
    v4u af = *(LAS v4u*)(L + L_ACT + ((r16 >> 2) * 32 + 8 * g) * 2);
    if (lane & 3) af = v4u{0u, 0u, 0u, 0u};
    f32x4 acc[2];
    acc[0] = mfma(Wf[0][2], af, f32x4{0.f, 0.f, 0.f, 0.f});
    acc[1] = mfma(Wf[1][2], af, f32x4{0.f, 0.f, 0.f, 0.f});
    SB();
#pragma unroll 1
    for (u32 it = 0; it < GSPIN; ++it) {
      o0 = UNTAG(o0, tag); o1 = UNTAG(o1, tag);
      const u32 bad = ((or4(o0) | or4(o1)) & 0xFFFu) | ((s0.x ^ s0.z) ^ H.x) | ((s0.y ^ s0.w) ^ H.y) | ((s1.x ^ s1.z) ^ H.x) | ((s1.y ^ s1.w) ^ H.y);
      if (__all(bad == 0u)) break;
      MEMFENCE();
      s0 = __builtin_amdgcn_raw_buffer_load_b128(rs, soff, 0, AUX_SC1);
      s1 = __builtin_amdgcn_raw_buffer_load_b128(rs, soff + 64, 0, AUX_SC1);
      o0 = __builtin_amdgcn_raw_buffer_load_b128(rs, ooff, 0, AUX_SC1);
      o1 = __builtin_amdgcn_raw_buffer_load_b128(rs, ooff + 16, 0, AUX_SC1);
    }
    float wgt;
    {
      const float m0 = asf(s0.x), m1 = asf(s1.x);
      float M = fmaxf(m0, m1);
      M = fmaxf(M, dpp<XOR1>(M)); M = fmaxf(M, dpp<XOR2>(M));
      const float e0 = __builtin_amdgcn_exp2f(m0 - M), e1 = __builtin_amdgcn_exp2f(m1 - M);
      float Ls = fmaf(e0, asf(s0.y), e1 * asf(s1.y));
      Ls += dpp<XOR1>(Ls); Ls += dpp<XOR2>(Ls);
      wgt = ((w >> 1) ? e1 : e0) * __builtin_amdgcn_rcpf(Ls);
    }
    {
      const f32x4 f0 = __builtin_bit_cast(f32x4, o0) * wgt, f1 = __builtin_bit_cast(f32x4, o1) * wgt;
      *(LAS v4u*)(L + ofw) = v4u{pk_bf16(f0[0], f0[1]), pk_bf16(f0[2], f0[3]), pk_bf16(f1[0], f1[1]), pk_bf16(f1[2], f1[3])};
    }
    LGKM0();
    __builtin_amdgcn_s_barrier();
    SB();
    {
      v4u of[4];
#pragma unroll
      for (int f = 0; f < 4; ++f) of[f] = *(LAS v4u*)(L + L_OF + (f * 64 + lane) * 16);
#pragma unroll
      for (int f = 0; f < 4; ++f) {
        acc[0] = mfma(Wf[0][f & 1], of[f], acc[0]);
        acc[1] = mfma(Wf[1][f & 1], of[f], acc[1]);
        SB();
      }
    }
#pragma unroll
    for (int tt = 0; tt < 2; ++tt)
#pragma unroll
      for (int i = 0; i < 4; ++i) { float v = acc[tt][i]; v += dpp<XOR1>(v); v += dpp<XOR2>(v); acc[tt][i] = v; }
    {
      const int u = lane & 3;
      const f32x4 v = u ? acc[1] : acc[0];
      if (u < 2 && (nu < 3 || w < 2))
        __builtin_amdgcn_raw_buffer_store_b128(tag4(v, tag), rs, p3off, 0, AUX_SC1);
    }
  } else if (w < 8) {
    const int wb = w - 4;
    __builtin_amdgcn_s_barrier();
    SB();
    if (wb < 2) { VM0(); lds_signal(flg, F_S1, wb, lane); } else lds_waitn(flg, F_S1, wb & 1);
    const int th = wb >> 1;
    if ((wb & 1) == 0) {
      lds_wait4(flg, F_XN);
      f32x4 acc[2] = {{0.f, 0.f, 0.f, 0.f}, {0.f, 0.f, 0.f, 0.f}};
#pragma unroll
      for (int kk = 0; kk < 4; ++kk) {
        v4u xf = *(LAS v4u*)(L + L_XN + (r16 >> 2) * (DM * 2) + (112 * b + 32 * kk + 8 * (kk == 3 ? (g & 1) : g)) * 2);
        if (kk == 3 && g >= 2) xf = v4u{0u, 0u, 0u, 0u};
        LAS unsigned char* p = L + L_WQ + (32 * kk + 8 * (kk == 3 ? (g & 1) : g) + (r16 >> 2)) * 128 + th * 64 + (lane & 3) * 8;
#pragma unroll
        for (int t = 0; t < 2; ++t) acc[t] = mfma(tr_frag(p + t * 32, p + t * 32 + 512), xf, acc[t]);
      }
      const f32x4 v = (lane & 1) ? acc[1] : acc[0];
      const u32 d0 = pk_bf16(v[0], v[1]), d1 = pk_bf16(v[2], v[3]);
      lds_waitn(flg, F_CLR, 0);
      const v2u H = *(LAS v2u*)(L + L_H);
      if ((lane & 2) == 0)
        __builtin_amdgcn_raw_buffer_store_b128(v4u{d0, d1, d0 ^ H.x, d1 ^ H.y}, rs,
                                               WREG(kvh) + (int)WR_QP + ((b * 4 + (r16 >> 2)) * 128 + 16 * s + 8 * th + 4 * (lane & 1) + g) * 16, 0, AUX_SC1);
    } else if (c < 224) {
      lds_wait4(flg, F_XN);
      f32x4 acc[2] = {{0.f, 0.f, 0.f, 0.f}, {0.f, 0.f, 0.f, 0.f}};
#pragma unroll
      for (int kk = 0; kk < 2; ++kk) {
        const int gg = (kk == 1 && g == 3) ? 0 : g;
        v4u xf = *(LAS v4u*)(L + L_XN + (r16 & 3) * (DM * 2) + (56 * s + 32 * kk + 8 * gg) * 2);
        if (kk == 1 && g == 3) xf = v4u{0u, 0u, 0u, 0u};
        LAS unsigned char* p = L + L_W1 + (32 * kk + 8 * gg + (r16 >> 2)) * 128 + th * 64 + (lane & 3) * 8;
#pragma unroll
        for (int t = 0; t < 2; ++t) acc[t] = mfma(xf, tr_frag(p + t * 32, p + t * 32 + 512), acc[t]);
      }
      const f32x4 v = (g & 1) ? acc[1] : acc[0];
      lds_waitn(flg, F_CLR, 0);
      if (lane < 32)
        __builtin_amdgcn_raw_buffer_store_b128(tag4(v, *(LAS u32*)(L + L_H + 8)), rs, WREG(s) + (int)WR_HP + (64 * pair + 32 * th + lane) * 16, 0, AUX_SC1);
    }
    lds_wait4(flg, F_KISS);
    const u16* vp = vc + ((size_t)pair * SK + (SK - WIN) + s * 128 + 32 * ((lane >> 3) & 1) + 8 * g) * DH + wb * 64 + (lane & 7) * 8;
    lds_waitn(flg, F_CLR, 0);
    u32 tagv = *(LAS u32*)(L + L_H + 8);
    int opub = WREG(kvh) + (int)WR_OP + (((g * 4 + wb) * 4 + b) * 8 + s) * 256 + (lane & 7) * 32 + ((lane >> 3) & 1) * 16;
    asm volatile("" : "+v"(tagv), "+v"(opub));
    v4u V[16];
#define VDEAL(p) do { _Pragma("unroll") for (int m_ = 0; m_ < 4; ++m_) { const u32 x_ = V[2 * (p)][m_], y_ = V[2 * (p) + 1][m_]; \
      V[2 * (p)][m_] = __builtin_amdgcn_perm(y_, x_, 0x05040100u); V[2 * (p) + 1][m_] = __builtin_amdgcn_perm(y_, x_, 0x07060302u); } } while (0)
#pragma unroll
    for (int i = 0; i < WINV; ++i) V[i] = ld_nt(vp + ((i >> 3) * 64 + (i & 7)) * DH);
    SB();
#pragma unroll
    for (int i = WINV; i < 16; ++i) {
      VMW(WINV - 1); V[i] = ld_nt(vp + ((i >> 3) * 64 + (i & 7)) * DH); SB();
      if (((i - WINV) & 1) == 1) { VDEAL((i - WINV) >> 1); SB(); }
    }
    lds_signal(flg, F_VISS, WI, lane);
    lds_wait4(flg, F_P);
    f32x4 o[8];
#pragma unroll
    for (int d = 0; d < 8; ++d) o[d] = f32x4{0.f, 0.f, 0.f, 0.f};
    {
      const v4u pf0 = *(LAS v4u*)(L + L_P + (((lane >> 2) & 1) * 128 + 32 * (lane & 1) + 8 * g) * 2);
      const v4u pf1 = *(LAS v4u*)(L + L_P + (((lane >> 2) & 1) * 128 + 64 + 32 * (lane & 1) + 8 * g) * 2);
      constexpr int PDONE = (16 - WINV) >> 1;
#pragma unroll
      for (int p = PDONE; p < 4; ++p) { VDEAL(p); SB(); }
#pragma unroll
      for (int d = 0; d < 8; ++d) o[d] = mfma(pf0, v4u{V[d & 1][d >> 1], V[2 + (d & 1)][d >> 1], V[4 + (d & 1)][d >> 1], V[6 + (d & 1)][d >> 1]}, o[d]);
      SB();
#pragma unroll
      for (int p = (PDONE > 4 ? PDONE : 4); p < 8; ++p) { VDEAL(p); SB(); }
#pragma unroll
      for (int d = 0; d < 8; ++d) o[d] = mfma(pf1, v4u{V[8 + (d & 1)][d >> 1], V[10 + (d & 1)][d >> 1], V[12 + (d & 1)][d >> 1], V[14 + (d & 1)][d >> 1]}, o[d]);
      SB();
    }
    {
      const bool aa = (lane >> 3) & 1;
      f32x4 ov;
#pragma unroll
      for (int i = 0; i < 4; ++i) {
        const float mine = aa ? o[4 + i][1] : o[i][0], other = aa ? o[i][1] : o[4 + i][0];
        ov[i] = mine + dpp<ROR8>(other);
      }
      if (lane < 32)
        __builtin_amdgcn_raw_buffer_store_b128(tag4(ov, tagv), rs, opub, 0, AUX_SC1);
    }
    lds_signal(flg, F_OPUB, WI, lane);
    __builtin_amdgcn_s_barrier();
  } else {
    const int kap = 8 * kvh + s;
    v2u Er = __builtin_amdgcn_raw_buffer_load_b64(rs, (int)WS_EPOCH, 0, AUX_SC1);
    const int rn = c % 28, rnu = c / 28, rb = rn / 7, rcol = 112 * rnu + 16 * (rn % 7) + 4 * (lane & 3);
    const v2u xres = *(const GAS v2u*)(x + (c < 112 ? rb * DM + rcol : 0));
    const int fl = (lane & 31) < 28 ? (lane & 31) : 27;
    const u32 b1v = b1[28 * kap + fl];
    SB();
    __builtin_amdgcn_s_barrier();
    SB();
    VM0();
    asm volatile("" : "+v"(Er));
    const u64 E = (((u64)Er.y << 32) | Er.x) + 1ull;
    const u64 H = (E * 0x9E3779B97F4A7C15ull) | 1ull;
    const u32 tag = (u32)E & 0xFFFu, Hlo = (u32)H, Hhi = (u32)(H >> 32);
    if (lane == 0) *(LAS v4u*)(L + L_H) = v4u{Hlo, Hhi, tag, 0u};
    lds_signal(flg, F_CLR, 0, lane);
    lds_wait4(flg, F_KISS);
    {
      const int off = WREG(lane >> 5) + (int)WR_HP + (28 * kap + fl) * 16;
      v4u hp[4];
#pragma unroll 1
      for (u32 it = 0; it < GSPIN; ++it) {
        MEMFENCE();
#pragma unroll
        for (int i = 0; i < 4; ++i) hp[i] = __builtin_amdgcn_raw_buffer_load_b128(rs, off + i * 2 * (int)WS_STRIDE, 0, AUX_SC1);
#pragma unroll
        for (int i = 0; i < 4; ++i) hp[i] = UNTAG(hp[i], tag);
        if (__all(((or4(hp[0]) | or4(hp[1]) | or4(hp[2]) | or4(hp[3])) & 0xFFFu) == 0u)) break;
        __builtin_amdgcn_s_sleep(2);
      }
      const f32x4 hs = (__builtin_bit_cast(f32x4, hp[0]) + __builtin_bit_cast(f32x4, hp[1])) + (__builtin_bit_cast(f32x4, hp[2]) + __builtin_bit_cast(f32x4, hp[3]));
      LAS u16* act = (LAS u16*)(L + L_ACT);
#pragma unroll
      for (int i = 0; i < 4; ++i) {
        const float hv = hs[i];
        auto t = __builtin_amdgcn_permlane32_swap(asu(hv), asu(hv), false, false);
        const float h = asf(t[0]) + asf(t[1]) + bf2f_lo(b1v);
        const float e = __expf(-fabsf(h));
        const float num = h >= 0.f ? fmaf(2.f, e, 1.f) : e * (e + 2.f);
        const float den = h >= 0.f ? fmaf(2.f * e, e, num) : num + 2.f;
        const float a = (lane & 31) < 28 ? h * (num / den) : 0.f;
        if (lane < 32) act[i * 32 + lane] = (u16)pk_bf16(a, a);
      }
      lds_signal(flg, F_ACT, 0, lane);
    }
    __builtin_amdgcn_s_barrier();
    if (c < 112) {
      __builtin_amdgcn_s_sleep(PDEL3);
      const int off = WREG(lane >> 5) + (int)WR_P3 + ((((lane >> 2) & 7) * 4 + rb) * 448 + rcol) * 4;
      v4u p[4];
#pragma unroll 1
      for (u32 it = 0; it < GSPIN; ++it) {
        MEMFENCE();
#pragma unroll
        for (int i = 0; i < 4; ++i) p[i] = __builtin_amdgcn_raw_buffer_load_b128(rs, off + i * 2 * (int)WS_STRIDE, 0, AUX_SC1);
#pragma unroll
        for (int i = 0; i < 4; ++i) p[i] = UNTAG(p[i], tag);
        if (__all(((or4(p[0]) | or4(p[1]) | or4(p[2]) | or4(p[3])) & 0xFFFu) == 0u)) break;
        __builtin_amdgcn_s_sleep(1);
      }
      f32x4 sum = (__builtin_bit_cast(f32x4, p[0]) + __builtin_bit_cast(f32x4, p[1])) + (__builtin_bit_cast(f32x4, p[2]) + __builtin_bit_cast(f32x4, p[3]));
#pragma unroll
      for (int i = 0; i < 4; ++i) { float v = sum[i]; v += dpp<ROR4>(v); v += dpp<ROR8>(v); sum[i] = xrow_sum(v); }
      if (lane < 4) {
        const v2u ov = {pk_bf16(sum[0] + bf2f_lo(xres.x), sum[1] + bf2f_hi(xres.x)), pk_bf16(sum[2] + bf2f_lo(xres.y), sum[3] + bf2f_hi(xres.y))};
        *(GAS v2u*)(out + rb * DM + rcol) = ov;
      }
      if (c == 0 && lane == 0) __hip_atomic_store((GAS u64*)(ws + WS_EPOCH), E, __ATOMIC_RELAXED, __HIP_MEMORY_SCOPE_AGENT);
    }
  }
  asm volatile(".Lfdk_code_end:" ::: "memory");
}

extern "C" void kernel_launch(void* const* d_in, const int* in_sizes, int n_in, void* d_out, int out_size, void* d_ws, size_t ws_size,
                              hipStream_t stream) {
  fused_decode_kernel<<<256, 576, 0, stream>>>((const u16*)d_in[0], (const u16*)d_in[1], (const u16*)d_in[2], (const u16*)d_in[3],
                                               (const u16*)d_in[4], (const u16*)d_in[6], (const u16*)d_in[7], (const u16*)d_in[8],
                                               (u16*)d_out, (unsigned char*)d_ws);
}
```
